```python
import torch

def _softmax(x, axis=-1):
    return torch.softmax(x, dim=axis)
def _gelu(x):
    return torch.nn.functional.gelu(x, approximate="tanh")
def _silu(x):
    return torch.nn.functional.silu(x)
def _sigmoid(x):
    return torch.sigmoid(x)
def _layernorm(x, axis=-1, eps=1e-5):
    m = x.mean(dim=axis, keepdim=True)
    v = x.var(dim=axis, keepdim=True, unbiased=False)
    return (x - m) / torch.sqrt(v + eps)
def _scatter_add(tgt, idx, src, axis=0):
    return tgt.clone().index_add_(axis, idx, src)
def _segment_sum(src, seg, n):
    out = torch.zeros((n,) + src.shape[1:], dtype=src.dtype)
    return out.index_add_(0, seg, src)
def _pad(x, pw):
    flat = tuple(v for pair in reversed(pw) for v in pair)
    return torch.nn.functional.pad(x, flat)

kernel_name = 'gated_mix__sliding_block_cached__glu_gated_reglu__decode'

def setup_inputs():

    _w = (lambda a: (lambda t: t.to(torch.bfloat16) if t.is_floating_point() else t)(torch.as_tensor(a)))
    def _A_setup():
        B, Sq, D = 4, 1, 512
        Hq, Hkv, Sk, Dh = 32, 8, 16384, 128
        assert Hq % Hkv == 0, (Hq, Hkv)
        return {
            "x": _w((lambda *s: torch.randn(*s, dtype=torch.bfloat16) * 0.05)(B, Sq, D)),
            "k_cache": _w((lambda *s: torch.randn(*s, dtype=torch.bfloat16) * 3.2)(B, Hkv, Sk, Dh)),
            "v_cache": _w((lambda *s: torch.randn(*s, dtype=torch.bfloat16) * 0.05)(B, Hkv, Sk, Dh)),
            "wq": _w((lambda *s: torch.randn(*s, dtype=torch.bfloat16) * 3.2)(D, Hq * Dh) * (D ** -0.5)),
            "wo": _w((lambda *s: torch.randn(*s, dtype=torch.bfloat16) * 0.05)(Hq * Dh, D) * ((Hq * Dh) ** -0.5)),
        }
    _A_in = _A_setup()

    def _B_setup():
        B, S, D, F = 4, 1, 512, 1792
        return {
            "x": _w((lambda *s: torch.randn(*s, dtype=torch.bfloat16) * 0.05)(B, S, D)),
            "W_gate": _w((lambda *s: torch.randn(*s, dtype=torch.bfloat16) * 0.2)(D, F) * (2.0 / D) ** 0.5),
            "W_up": _w((lambda *s: torch.randn(*s, dtype=torch.bfloat16) * 0.2)(D, F) * (2.0 / D) ** 0.5),
            "W_down": _w((lambda *s: torch.randn(*s, dtype=torch.bfloat16) * 0.2)(F, D) * (1.0 / F) ** 0.5),
        }
    _B_in = _B_setup()

    return {
        "A_x": _A_in["x"],
        "A_k_cache": _A_in["k_cache"],
        "A_v_cache": _A_in["v_cache"],
        "A_wq": _A_in["wq"],
        "A_wo": _A_in["wo"],
        "B_x": _B_in["x"],
        "B_W_gate": _B_in["W_gate"],
        "B_W_up": _B_in["W_up"],
        "B_W_down": _B_in["W_down"],
        "gate_w": _w((lambda *s: torch.randn(*s, dtype=torch.bfloat16) * 0.05)(512) * 16.0),
    }

def reference(A_x, A_k_cache, A_v_cache, A_wq, A_wo, B_x, B_W_gate, B_W_up, B_W_down, gate_w):
    def _A_ref(x, k_cache, v_cache, wq, wo):
        B, Sq, D = x.shape
        Hq, Dh = 32, 128
        Hkv, Sk = k_cache.shape[1], k_cache.shape[2]
        W = min(4096, max(256, Sk // 4))
        rep = Hq // Hkv
        q = (lambda x, *d: x.permute(*d))((x @ wq).reshape(B, Sq, Hq, Dh), 0, 2, 1, 3)
        k = (lambda x, n, axis: torch.repeat_interleave(x, n, dim=axis))(k_cache[:, :, Sk - W:, :], rep, axis=1)
        v = (lambda x, n, axis: torch.repeat_interleave(x, n, dim=axis))(v_cache[:, :, Sk - W:, :], rep, axis=1)
        scores = torch.einsum("bhqd,bhkd->bhqk", q, k) * (Dh ** -0.5)
        p = _softmax(scores, axis=-1)
        o = torch.einsum("bhqk,bhkd->bhqd", p, v)
        return (lambda x, *d: x.permute(*d))(o, 0, 2, 1, 3).reshape(B, Sq, Hq * Dh) @ wo

    def _B_ref(x, W_gate, W_up, W_down):
        gate = torch.einsum("bsd,df->bsf", x, W_gate)
        up = torch.einsum("bsd,df->bsf", x, W_up)
        gate = gate * (lambda x, dt: x.to(dt))(gate > 0, gate.dtype)
        h = gate * up
        return torch.einsum("bsf,fd->bsd", h, W_down)

    x = A_x
    g = _sigmoid((x * gate_w).sum(-1, keepdims=True))
    a = _A_ref(x, A_k_cache, A_v_cache, A_wq, A_wo)
    b = _B_ref(x, B_W_gate, B_W_up, B_W_down)
    return g * a + (1 - g) * b
```

```cpp
#include <hip/hip_runtime.h>
#include <hip/hip_bf16.h>
#include <stdint.h>

typedef __hip_bfloat16 bf16;
typedef unsigned int u32;
typedef unsigned long long u64;
typedef u32 u32x4 __attribute__((ext_vector_type(4)));
typedef float f32x4 __attribute__((ext_vector_type(4)));
typedef __bf16 bf16x2 __attribute__((ext_vector_type(2)));
#define GAS __attribute__((address_space(1)))
typedef GAS u64 gu64;

constexpr int NB = 4, DM = 512, HQ = 32, HKV = 8, SK = 16384, DH = 128, WIN = 4096, FF = 1792;
constexpr int NSPLIT = 8, KEYS_WG = WIN / NSPLIT;
constexpr int NWAVE = 8, KEYS_WAVE = KEYS_WG / NWAVE;
constexpr int NWG = NB * HKV * NSPLIT;

constexpr size_t WS_Q = 0;
constexpr size_t WS_H = WS_Q + (size_t)NB * HQ * DH * 2;
constexpr size_t WS_ZERO = 64 * 1024;
constexpr size_t WS_ML = WS_ZERO;
constexpr size_t ML_BYTES = (size_t)32 * NSPLIT * 4 * 2 * 8;
constexpr size_t WS_P1 = WS_ML + ML_BYTES;
constexpr size_t P1_BYTES = (size_t)32 * NSPLIT * 4 * DH * 8;
constexpr size_t WS_P2 = WS_P1 + P1_BYTES;
constexpr size_t P2_BYTES = (size_t)NB * 64 * DM * 8;
constexpr size_t ZERO_BYTES = ML_BYTES + P1_BYTES + P2_BYTES;
constexpr int PRE_QBLK = HQ * DH / 16;
constexpr int PRE_HBLK = FF / 16;
constexpr int PRE_BLOCKS = PRE_QBLK + PRE_HBLK;
constexpr u32 TAG = 0x5A5A0001u;
constexpr u32 SPIN_CAP = 1u << 20;

#define RLX_AGENT __ATOMIC_RELAXED, __HIP_MEMORY_SCOPE_AGENT

__device__ __forceinline__ float bf2f(u32 lo16) { return __uint_as_float(lo16 << 16); }
__device__ __forceinline__ float bflo(u32 p) { return __uint_as_float(p << 16); }
__device__ __forceinline__ float bfhi(u32 p) { return __uint_as_float(p & 0xffff0000u); }
__device__ __forceinline__ u32 f2bf_bits(float f) {
  bf16 h = __float2bfloat16(f);
  return (u32)__builtin_bit_cast(unsigned short, h);
}
__device__ __forceinline__ float bfround(float f) { return __uint_as_float(f2bf_bits(f) << 16); }
__device__ __forceinline__ float dot2(u32 a, u32 b, float c) {
  return __builtin_amdgcn_fdot2_f32_bf16(__builtin_bit_cast(bf16x2, a), __builtin_bit_cast(bf16x2, b), c, false);
}
template <int CTRL> __device__ __forceinline__ float dpp(float x) {
  return __builtin_bit_cast(float, __builtin_amdgcn_mov_dpp(__builtin_bit_cast(int, x), CTRL, 0xf, 0xf, true));
}
constexpr int XOR1 = 0xB1, XOR2 = 0x4E, XOR7 = 0x141, XOR8 = 0x128, ROR4 = 0x124;
__device__ __forceinline__ float xrow16_max(float x) {
  auto s = __builtin_amdgcn_permlane16_swap(__float_as_uint(x), __float_as_uint(x), false, false);
  x = fmaxf(__uint_as_float(s[0]), __uint_as_float(s[1]));
  auto t = __builtin_amdgcn_permlane32_swap(__float_as_uint(x), __float_as_uint(x), false, false);
  return fmaxf(__uint_as_float(t[0]), __uint_as_float(t[1]));
}
__device__ __forceinline__ float xrow16_sum(float x) {
  auto s = __builtin_amdgcn_permlane16_swap(__float_as_uint(x), __float_as_uint(x), false, false);
  x = __uint_as_float(s[0]) + __uint_as_float(s[1]);
  auto t = __builtin_amdgcn_permlane32_swap(__float_as_uint(x), __float_as_uint(x), false, false);
  return __uint_as_float(t[0]) + __uint_as_float(t[1]);
}
__device__ __forceinline__ float wave_sum(float x) {
  x += dpp<XOR1>(x); x += dpp<XOR2>(x); x += dpp<XOR7>(x); x += dpp<XOR8>(x);
  return xrow16_sum(x);
}
__device__ __forceinline__ u32x4 ld16(const void* p) { return *reinterpret_cast<const u32x4*>(p); }
__device__ __forceinline__ u32x4 ld16nt(const void* p) { return __builtin_nontemporal_load(reinterpret_cast<const u32x4*>(p)); }
__device__ __forceinline__ void st_gran(u64* g, float v) {
  __hip_atomic_store((gu64*)g, ((u64)TAG << 32) | (u64)__float_as_uint(v), RLX_AGENT);
}
__device__ __forceinline__ u64 ld_gran(const u64* g) { return __hip_atomic_load((gu64*)g, RLX_AGENT); }

__device__ __forceinline__ float tr_reduce64(float (&v)[64], int lane) {
  const int c = lane & 15;
  float a[32];
#pragma unroll
  for (int j = 0; j < 32; ++j) {
    auto s = __builtin_amdgcn_permlane32_swap(__float_as_uint(v[j]), __float_as_uint(v[j + 32]), false, false);
    a[j] = __uint_as_float(s[0]) + __uint_as_float(s[1]);
  }
  float b2[16];
#pragma unroll
  for (int j = 0; j < 16; ++j) {
    auto s = __builtin_amdgcn_permlane16_swap(__float_as_uint(a[j]), __float_as_uint(a[j + 16]), false, false);
    b2[j] = __uint_as_float(s[0]) + __uint_as_float(s[1]);
  }
#define TRS(HALF, CTRL, BIT) _Pragma("unroll") for (int h = 0; h < (HALF); ++h) { \
    const float lo_ = b2[h], hi_ = b2[h + (HALF)]; const float t1_ = lo_ + dpp<CTRL>(lo_), t2_ = hi_ + dpp<CTRL>(hi_); \
    b2[h] = (c & (BIT)) ? t2_ : t1_; }
  TRS(8, XOR8, 8) TRS(4, XOR7, 4) TRS(2, XOR2, 2) TRS(1, XOR1, 1)
#undef TRS
  return b2[0];
}

__device__ __forceinline__ float slice16(const bf16* __restrict__ w, int ldw, int col0, const float (&xv)[4],
                                          float* red, int tid) {
  const int lane = tid & 63, wave = tid >> 6;
  const bf16* p = w + (size_t)tid * ldw + col0;
  const u32x4 w0 = ld16nt(p), w1 = ld16nt(p + 8);
  float v[64];
#pragma unroll
  for (int b = 0; b < 4; ++b) {
#pragma unroll
    for (int j = 0; j < 4; ++j) {
      v[b * 16 + 2 * j] = xv[b] * bflo(w0[j]);
      v[b * 16 + 2 * j + 1] = xv[b] * bfhi(w0[j]);
      v[b * 16 + 8 + 2 * j] = xv[b] * bflo(w1[j]);
      v[b * 16 + 8 + 2 * j + 1] = xv[b] * bfhi(w1[j]);
    }
  }
  const float r = tr_reduce64(v, lane);
  red[wave * 64 + lane] = r;
  __syncthreads();
  float s = 0.f;
  if (tid < 64) {
#pragma unroll
    for (int wv = 0; wv < 8; ++wv) s += red[wv * 64 + tid];
  }
  __syncthreads();
  return s;
}

__global__ void __launch_bounds__(512) k_pre(const bf16* __restrict__ x, const bf16* __restrict__ wq,
                                              const bf16* __restrict__ wg, const bf16* __restrict__ wu,
                                              unsigned char* __restrict__ ws) {
  __shared__ __attribute__((aligned(16))) float red[512];
  const int tid = threadIdx.x, blk = blockIdx.x;
  {
    u32x4* z = reinterpret_cast<u32x4*>(ws + WS_ZERO);
    const int n16 = (int)(ZERO_BYTES / 16);
    for (int i = blk * 512 + tid; i < n16; i += PRE_BLOCKS * 512) z[i] = u32x4{0u, 0u, 0u, 0u};
  }
  float xv[4];
#pragma unroll
  for (int b = 0; b < 4; ++b) xv[b] = __bfloat162float(x[b * DM + tid]);
  if (blk < PRE_QBLK) {
    const float s = slice16(wq, HQ * DH, blk * 16, xv, red, tid);
    if (tid < 64) {
      bf16* q = reinterpret_cast<bf16*>(ws + WS_Q);
      q[(tid >> 4) * (HQ * DH) + blk * 16 + (tid & 15)] = __float2bfloat16(s);
    }
  } else {
    const int hb = blk - PRE_QBLK;
    const float g = slice16(wg, FF, hb * 16, xv, red, tid);
    const float u = slice16(wu, FF, hb * 16, xv, red, tid);
    if (tid < 64) {
      bf16* h = reinterpret_cast<bf16*>(ws + WS_H);
      const float gr = fmaxf(bfround(g), 0.f), ur = bfround(u);
      h[(tid >> 4) * FF + hb * 16 + (tid & 15)] = __float2bfloat16(gr * ur);
    }
  }
}

constexpr int L_P = 0;
constexpr int L_MRG = L_P + 2048;
constexpr int MREC = 132;
constexpr int L_OB = L_MRG + NWAVE * 4 * MREC;
constexpr int L_RED = L_OB + 64;
constexpr int L_TOTAL = L_RED + 4096;

__global__ void __launch_bounds__(512, 2) k_main(const bf16* __restrict__ x, const bf16* __restrict__ kc,
                                                  const bf16* __restrict__ vc, const bf16* __restrict__ wo,
                                                  const bf16* __restrict__ wd, const bf16* __restrict__ gw,
                                                  bf16* __restrict__ out, unsigned char* __restrict__ ws) {
  __shared__ __attribute__((aligned(16))) float lds[L_TOTAL];
  const int tid = threadIdx.x, lane = tid & 63, wave = __builtin_amdgcn_readfirstlane(tid >> 6);
  const int r = lane >> 4, c = lane & 15;
  const int id = blockIdx.x, kvh = id & 7, b = (id >> 3) >> 3, s = (id >> 3) & 7;
  const int grp = b * 8 + kvh, src = kvh * 8 + s;
  constexpr float C2 = 0.088388347648318440f * 1.4426950408889634f;

  const bf16* qp = reinterpret_cast<const bf16*>(ws + WS_Q) + (size_t)b * (HQ * DH) + kvh * 4 * DH + 8 * c;
  u32x4 q[4];
#pragma unroll
  for (int g = 0; g < 4; ++g) q[g] = ld16(qp + g * DH);
  const size_t kvoff = ((size_t)(b * HKV + kvh) * SK + (SK - WIN) + s * KEYS_WG + wave * KEYS_WAVE + r) * DH + 8 * c;
  u32x4 K[16], V[16];
#pragma unroll
  for (int i = 0; i < 16; ++i) K[i] = ld16nt(kc + kvoff + (size_t)i * 4 * DH);
#pragma unroll
  for (int i = 0; i < 16; ++i) V[i] = ld16nt(vc + kvoff + (size_t)i * 4 * DH);

  float sc[4];
#pragma unroll
  for (int pg = 0; pg < 4; ++pg) {
    float xx[16];
#pragma unroll
    for (int t = 0; t < 4; ++t)
#pragma unroll
      for (int g = 0; g < 4; ++g) {
        float d = 0.f;
#pragma unroll
        for (int k = 0; k < 4; ++k) d = dot2(K[pg * 4 + t][k], q[g][k], d);
        xx[t * 4 + g] = d;
      }
#define TRS(HALF, CTRL, BIT) _Pragma("unroll") for (int h = 0; h < (HALF); ++h) { \
    const float lo_ = xx[h], hi_ = xx[h + (HALF)]; const float t1_ = lo_ + dpp<CTRL>(lo_), t2_ = hi_ + dpp<CTRL>(hi_); \
    xx[h] = (c & (BIT)) ? t2_ : t1_; }
    TRS(8, XOR8, 8) TRS(4, XOR7, 4) TRS(2, XOR2, 2) TRS(1, XOR1, 1)
#undef TRS
    sc[pg] = xx[0] * C2;
  }
  const int hs = s >> 1, half = s & 1;
  const bf16* wop = wo + ((size_t)((kvh * 4 + hs) * DH + half * 64 + wave * 8)) * DM + 8 * lane;
  u32x4 WO[8];
#pragma unroll
  for (int i = 0; i < 8; ++i) WO[i] = ld16nt(wop + (size_t)i * DM);
  const int drow0 = src * 28 + (wave < 7 ? wave * 4 : 0);
  u32x4 WD[4];
#pragma unroll
  for (int i = 0; i < 4; ++i) WD[i] = ld16nt(wd + (size_t)(drow0 + i) * DM + 8 * lane);
  const bf16* hp = reinterpret_cast<const bf16*>(ws + WS_H) + (size_t)b * FF + drow0;
  float hv[4];
#pragma unroll
  for (int i = 0; i < 4; ++i) hv[i] = __bfloat162float(hp[i]);
  float gsum = __bfloat162float(x[b * DM + tid]) * __bfloat162float(gw[tid]);

  float m = fmaxf(fmaxf(sc[0], sc[1]), fmaxf(sc[2], sc[3]));
  m = fmaxf(m, dpp<ROR4>(m)); m = fmaxf(m, dpp<XOR8>(m)); m = xrow16_max(m);
  float l = 0.f;
#pragma unroll
  for (int pg = 0; pg < 4; ++pg) {
    const float p = __builtin_amdgcn_exp2f(sc[pg] - m);
    l += p;
    lds[L_P + wave * 256 + (16 * pg + 4 * (c >> 2) + r) * 4 + (c & 3)] = p;
  }
  l += dpp<ROR4>(l); l += dpp<XOR8>(l); l = xrow16_sum(l);
  asm volatile("s_waitcnt lgkmcnt(0)" ::: "memory");
  float acc[32];
#pragma unroll
  for (int k = 0; k < 32; ++k) acc[k] = 0.f;
#pragma unroll
  for (int i = 0; i < 16; ++i) {
    const f32x4 p = *reinterpret_cast<const f32x4*>(&lds[L_P + wave * 256 + (4 * i + r) * 4]);
    float vf[8];
#pragma unroll
    for (int k = 0; k < 4; ++k) { vf[2 * k] = bflo(V[i][k]); vf[2 * k + 1] = bfhi(V[i][k]); }
#pragma unroll
    for (int g = 0; g < 4; ++g)
#pragma unroll
      for (int k = 0; k < 8; ++k) acc[g * 8 + k] = fmaf(p[g], vf[k], acc[g * 8 + k]);
  }
#pragma unroll
  for (int k = 0; k < 32; ++k) acc[k] = xrow16_sum(acc[k]);
  {
    float* mr = &lds[L_MRG + wave * 4 * MREC];
    if (r == 0) {
#pragma unroll
      for (int g = 0; g < 4; ++g) {
        *reinterpret_cast<f32x4*>(mr + g * MREC + 4 + 8 * c) = f32x4{acc[g * 8], acc[g * 8 + 1], acc[g * 8 + 2], acc[g * 8 + 3]};
        *reinterpret_cast<f32x4*>(mr + g * MREC + 8 + 8 * c) = f32x4{acc[g * 8 + 4], acc[g * 8 + 5], acc[g * 8 + 6], acc[g * 8 + 7]};
      }
      if (c < 4) { mr[c * MREC] = m; mr[c * MREC + 1] = l; }
    }
  }
  gsum = wave_sum(gsum);
  if (lane == 0) lds[L_RED + wave] = gsum;
  __syncthreads();
  float gate;
  {
    float t = 0.f;
#pragma unroll
    for (int wv = 0; wv < 8; ++wv) t += lds[L_RED + wv];
    gate = 1.f / (1.f + __expf(-t));
  }
  u64* P1 = reinterpret_cast<u64*>(ws + WS_P1);
  u64* ML = reinterpret_cast<u64*>(ws + WS_ML);
  {
    const int g = tid >> 7, d = tid & 127;
    float M = lds[L_MRG + g * MREC];
#pragma unroll
    for (int wv = 1; wv < 8; ++wv) M = fmaxf(M, lds[L_MRG + (wv * 4 + g) * MREC]);
    float L = 0.f, o = 0.f;
#pragma unroll
    for (int wv = 0; wv < 8; ++wv) {
      const float* rec = &lds[L_MRG + (wv * 4 + g) * MREC];
      const float wt = __builtin_amdgcn_exp2f(rec[0] - M);
      L = fmaf(wt, rec[1], L); o = fmaf(wt, rec[4 + d], o);
    }
    st_gran(P1 + ((size_t)(grp * NSPLIT + s) * 4 + g) * DH + d, o);
    if (d == 0) {
      st_gran(ML + ((size_t)(grp * NSPLIT + s) * 4 + g) * 2, M);
      st_gran(ML + ((size_t)(grp * NSPLIT + s) * 4 + g) * 2 + 1, L);
    }
  }
  if (wave == 0) {
    const u64* g1 = P1 + ((size_t)(grp * NSPLIT) * 4 + hs) * DH + half * 64 + lane;
    const u64* g2 = ML + ((size_t)(grp * NSPLIT + (lane >> 1 & 7)) * 4 + hs) * 2 + (lane & 1);
    float a[8], ml = 0.f;
    for (u32 spins = 0; spins < SPIN_CAP; ++spins) {
      bool ok = true;
#pragma unroll
      for (int j = 0; j < 8; ++j) {
        const u64 v = ld_gran(g1 + (size_t)j * 4 * DH);
        a[j] = __uint_as_float((u32)v); ok &= (u32)(v >> 32) == TAG;
      }
      const u64 v = ld_gran(g2);
      ml = __uint_as_float((u32)v); ok &= (u32)(v >> 32) == TAG;
      if (__all(ok)) break;
      __builtin_amdgcn_s_sleep(1);
    }
    float M = -INFINITY;
#pragma unroll
    for (int j = 0; j < 8; ++j) M = fmaxf(M, __shfl(ml, 2 * j));
    float L = 0.f, o = 0.f;
#pragma unroll
    for (int j = 0; j < 8; ++j) {
      const float wt = __builtin_amdgcn_exp2f(__shfl(ml, 2 * j) - M);
      L = fmaf(wt, __shfl(ml, 2 * j + 1), L); o = fmaf(wt, a[j], o);
    }
    lds[L_OB + lane] = bfround(o / L) * gate;
  }
  __syncthreads();
  float pr[8];
#pragma unroll
  for (int k = 0; k < 8; ++k) pr[k] = 0.f;
#pragma unroll
  for (int i = 0; i < 8; ++i) {
    const float ov = lds[L_OB + wave * 8 + i];
#pragma unroll
    for (int k = 0; k < 4; ++k) {
      pr[2 * k] = fmaf(ov, bflo(WO[i][k]), pr[2 * k]);
      pr[2 * k + 1] = fmaf(ov, bfhi(WO[i][k]), pr[2 * k + 1]);
    }
  }
  if (wave < 7) {
#pragma unroll
    for (int i = 0; i < 4; ++i) {
      const float hvv = hv[i] * (1.f - gate);
#pragma unroll
      for (int k = 0; k < 4; ++k) {
        pr[2 * k] = fmaf(hvv, bflo(WD[i][k]), pr[2 * k]);
        pr[2 * k + 1] = fmaf(hvv, bfhi(WD[i][k]), pr[2 * k + 1]);
      }
    }
  }
  *reinterpret_cast<f32x4*>(&lds[L_RED + wave * 512 + 8 * lane]) = f32x4{pr[0], pr[1], pr[2], pr[3]};
  *reinterpret_cast<f32x4*>(&lds[L_RED + wave * 512 + 8 * lane + 4]) = f32x4{pr[4], pr[5], pr[6], pr[7]};
  __syncthreads();
  u64* P2 = reinterpret_cast<u64*>(ws + WS_P2);
  {
    float t = 0.f;
#pragma unroll
    for (int wv = 0; wv < 8; ++wv) t += lds[L_RED + wv * 512 + tid];
    st_gran(P2 + ((size_t)(b * 64 + src)) * DM + tid, t);
  }
  if (wave == 0) {
    const u64* g = P2 + ((size_t)(b * 64 + lane)) * DM + 8 * src;
    float a[8];
    for (u32 spins = 0; spins < SPIN_CAP; ++spins) {
      bool ok = true;
#pragma unroll
      for (int j = 0; j < 8; ++j) {
        const u64 v = ld_gran(g + j);
        a[j] = __uint_as_float((u32)v); ok &= (u32)(v >> 32) == TAG;
      }
      if (__all(ok)) break;
      __builtin_amdgcn_s_sleep(1);
    }
#pragma unroll
    for (int j = 0; j < 8; ++j) a[j] = wave_sum(a[j]);
    if (lane == 0) {
      u32x4 o;
#pragma unroll
      for (int k = 0; k < 4; ++k) o[k] = f2bf_bits(a[2 * k]) | (f2bf_bits(a[2 * k + 1]) << 16);
      *reinterpret_cast<u32x4*>(out + (size_t)b * DM + 8 * src) = o;
    }
  }
}

extern "C" void kernel_launch(void* const* d_in, const int* in_sizes, int n_in,
                              void* d_out, int out_size, void* d_ws, size_t ws_size,
                              hipStream_t stream) {
  const bf16* x = (const bf16*)d_in[0];
  const bf16* kc = (const bf16*)d_in[1];
  const bf16* vc = (const bf16*)d_in[2];
  const bf16* wq = (const bf16*)d_in[3];
  const bf16* wo = (const bf16*)d_in[4];
  const bf16* wg = (const bf16*)d_in[6];
  const bf16* wu = (const bf16*)d_in[7];
  const bf16* wd = (const bf16*)d_in[8];
  const bf16* gw = (const bf16*)d_in[9];
  unsigned char* ws = (unsigned char*)d_ws;
  k_pre<<<PRE_BLOCKS, 512, 0, stream>>>(x, wq, wg, wu, ws);
  k_main<<<NWG, 512, 0, stream>>>(x, kc, vc, wo, wd, gw, (bf16*)d_out, ws);
}
```

```cpp
#include <hip/hip_runtime.h>
#include <hip/hip_bf16.h>
#include <stdint.h>

typedef __hip_bfloat16 bf16;
typedef unsigned int u32;
typedef unsigned long long u64;
typedef u32 u32x4 __attribute__((ext_vector_type(4)));
typedef float f32x4 __attribute__((ext_vector_type(4)));
typedef __bf16 bf16x2 __attribute__((ext_vector_type(2)));
#define GAS __attribute__((address_space(1)))
typedef GAS u64 gu64;

constexpr int NB = 4, DM = 512, HQ = 32, HKV = 8, SK = 16384, DH = 128, WIN = 4096, FF = 1792;
constexpr int NSPLIT = 8, KEYS_WG = WIN / NSPLIT;
constexpr int NWAVE = 8, KEYS_WAVE = KEYS_WG / NWAVE;
constexpr int NWG = NB * HKV * NSPLIT;

constexpr size_t WS_Q = 0;
constexpr size_t WS_H = WS_Q + (size_t)NB * HQ * DH * 2;
constexpr size_t WS_ZERO = 64 * 1024;
constexpr size_t WS_ML = WS_ZERO;
constexpr size_t ML_BYTES = (size_t)32 * NSPLIT * 4 * 2 * 8;
constexpr size_t WS_P1 = WS_ML + ML_BYTES;
constexpr size_t P1_BYTES = (size_t)32 * NSPLIT * 4 * DH * 8;
constexpr size_t WS_P2 = WS_P1 + P1_BYTES;
constexpr size_t P2_BYTES = (size_t)NB * 64 * DM * 8;
constexpr size_t ZERO_BYTES = ML_BYTES + P1_BYTES + P2_BYTES;
constexpr int PRE_QBLK = HQ * DH / 16;
constexpr int PRE_HBLK = FF / 8;
constexpr int PRE_BLOCKS = PRE_QBLK + PRE_HBLK;
constexpr u32 TAG = 0x5A5A0001u;
constexpr u32 SPIN_CAP = 1u << 20;

#define RLX_AGENT __ATOMIC_RELAXED, __HIP_MEMORY_SCOPE_AGENT

__device__ __forceinline__ float bf2f(u32 lo16) { return __uint_as_float(lo16 << 16); }
__device__ __forceinline__ float bflo(u32 p) { return __uint_as_float(p << 16); }
__device__ __forceinline__ float bfhi(u32 p) { return __uint_as_float(p & 0xffff0000u); }
__device__ __forceinline__ u32 f2bf_bits(float f) {
  bf16 h = __float2bfloat16(f);
  return (u32)__builtin_bit_cast(unsigned short, h);
}
__device__ __forceinline__ float bfround(float f) { return __uint_as_float(f2bf_bits(f) << 16); }
__device__ __forceinline__ float dot2(u32 a, u32 b, float c) {
  return __builtin_amdgcn_fdot2_f32_bf16(__builtin_bit_cast(bf16x2, a), __builtin_bit_cast(bf16x2, b), c, false);
}
template <int CTRL> __device__ __forceinline__ float dpp(float x) {
  return __builtin_bit_cast(float, __builtin_amdgcn_mov_dpp(__builtin_bit_cast(int, x), CTRL, 0xf, 0xf, true));
}
constexpr int XOR1 = 0xB1, XOR2 = 0x4E, XOR7 = 0x141, XOR8 = 0x128, ROR4 = 0x124;
__device__ __forceinline__ float xrow16_max(float x) {
  auto s = __builtin_amdgcn_permlane16_swap(__float_as_uint(x), __float_as_uint(x), false, false);
  x = fmaxf(__uint_as_float(s[0]), __uint_as_float(s[1]));
  auto t = __builtin_amdgcn_permlane32_swap(__float_as_uint(x), __float_as_uint(x), false, false);
  return fmaxf(__uint_as_float(t[0]), __uint_as_float(t[1]));
}
__device__ __forceinline__ float xrow16_sum(float x) {
  auto s = __builtin_amdgcn_permlane16_swap(__float_as_uint(x), __float_as_uint(x), false, false);
  x = __uint_as_float(s[0]) + __uint_as_float(s[1]);
  auto t = __builtin_amdgcn_permlane32_swap(__float_as_uint(x), __float_as_uint(x), false, false);
  return __uint_as_float(t[0]) + __uint_as_float(t[1]);
}
__device__ __forceinline__ float wave_sum(float x) {
  x += dpp<XOR1>(x); x += dpp<XOR2>(x); x += dpp<XOR7>(x); x += dpp<XOR8>(x);
  return xrow16_sum(x);
}
__device__ __forceinline__ u32x4 ld16(const void* p) { return *reinterpret_cast<const u32x4*>(p); }
__device__ __forceinline__ u32x4 ld16nt(const void* p) { return __builtin_nontemporal_load(reinterpret_cast<const u32x4*>(p)); }
__device__ __forceinline__ void st_gran(u64* g, float v) {
  __hip_atomic_store((gu64*)g, ((u64)TAG << 32) | (u64)__float_as_uint(v), RLX_AGENT);
}
__device__ __forceinline__ u64 ld_gran(const u64* g) { return __hip_atomic_load((gu64*)g, RLX_AGENT); }

#define TRS_DPP(ARR, HALF, CTRL, BIT, LANE) _Pragma("unroll") for (int h = 0; h < (HALF); ++h) { \
    const float lo_ = ARR[h], hi_ = ARR[h + (HALF)]; const float t1_ = lo_ + dpp<CTRL>(lo_), t2_ = hi_ + dpp<CTRL>(hi_); \
    ARR[h] = ((LANE) & (BIT)) ? t2_ : t1_; }
#define TRS_P32(ARR, HALF) _Pragma("unroll") for (int h = 0; h < (HALF); ++h) { \
    auto s_ = __builtin_amdgcn_permlane32_swap(__float_as_uint(ARR[h]), __float_as_uint(ARR[h + (HALF)]), false, false); \
    ARR[h] = __uint_as_float(s_[0]) + __uint_as_float(s_[1]); }
#define TRS_P16(ARR, HALF) _Pragma("unroll") for (int h = 0; h < (HALF); ++h) { \
    auto s_ = __builtin_amdgcn_permlane16_swap(__float_as_uint(ARR[h]), __float_as_uint(ARR[h + (HALF)]), false, false); \
    ARR[h] = __uint_as_float(s_[0]) + __uint_as_float(s_[1]); }

__global__ void __launch_bounds__(512) k_pre(const bf16* __restrict__ x, const bf16* __restrict__ wq,
                                              const bf16* __restrict__ wg, const bf16* __restrict__ wu,
                                              unsigned char* __restrict__ ws) {
  __shared__ __attribute__((aligned(16))) float red[512];
  const int tid = threadIdx.x, lane = tid & 63, wave = tid >> 6;
  const bool isq = blockIdx.x < PRE_QBLK;
  const int blk = isq ? (blockIdx.x & 7) * 32 + (blockIdx.x >> 3) : ((blockIdx.x - PRE_QBLK) & 7) * 28 + ((blockIdx.x - PRE_QBLK) >> 3);
  float v[32];
  if (isq) {
    const int r0 = ((tid >> 1) + blk * 32) & 255;
    const bf16* p = wq + (size_t)r0 * (HQ * DH) + blk * 16 + (tid & 1) * 8;
    const u32x4 w0 = ld16nt(p), w1 = ld16nt(p + (size_t)256 * (HQ * DH));
#pragma unroll
    for (int b = 0; b < 4; ++b) {
      const float x0 = __bfloat162float(x[b * DM + r0]), x1 = __bfloat162float(x[b * DM + r0 + 256]);
#pragma unroll
      for (int j = 0; j < 4; ++j) {
        v[b * 8 + 2 * j] = x0 * bflo(w0[j]) + x1 * bflo(w1[j]);
        v[b * 8 + 2 * j + 1] = x0 * bfhi(w0[j]) + x1 * bfhi(w1[j]);
      }
    }
  } else {
    const int r0 = (tid + blk * 64) & 511;
    const u32x4 w0 = ld16nt(wg + (size_t)r0 * FF + blk * 8), w1 = ld16nt(wu + (size_t)r0 * FF + blk * 8);
#pragma unroll
    for (int b = 0; b < 4; ++b) {
      const float x0 = __bfloat162float(x[b * DM + r0]);
#pragma unroll
      for (int j = 0; j < 4; ++j) {
        v[b * 8 + 2 * j] = x0 * bflo(w0[j]); v[b * 8 + 2 * j + 1] = x0 * bfhi(w0[j]);
      }
    }
    float u[32];
#pragma unroll
    for (int b = 0; b < 4; ++b) {
      const float x0 = __bfloat162float(x[b * DM + r0]);
#pragma unroll
      for (int j = 0; j < 4; ++j) {
        u[b * 8 + 2 * j] = x0 * bflo(w1[j]); u[b * 8 + 2 * j + 1] = x0 * bfhi(w1[j]);
      }
    }
    float a[32];
#pragma unroll
    for (int h = 0; h < 32; ++h) {
      auto s_ = __builtin_amdgcn_permlane32_swap(__float_as_uint(v[h]), __float_as_uint(u[h]), false, false);
      a[h] = __uint_as_float(s_[0]) + __uint_as_float(s_[1]);
    }
    TRS_P16(a, 16) TRS_DPP(a, 8, XOR8, 8, lane) TRS_DPP(a, 4, XOR7, 4, lane) TRS_DPP(a, 2, XOR2, 2, lane) TRS_DPP(a, 1, XOR1, 1, lane)
    red[wave * 64 + lane] = a[0];
  }
  if (isq) {
    TRS_P32(v, 16) TRS_P16(v, 8) TRS_DPP(v, 4, XOR8, 8, lane)
#pragma unroll
    for (int h = 0; h < 2; ++h) {
      const float lo_ = v[h], hi_ = v[h + 2];
      const float t1_ = lo_ + __shfl_xor(lo_, 4), t2_ = hi_ + __shfl_xor(hi_, 4);
      v[h] = (lane & 4) ? t2_ : t1_;
    }
    {
      const float lo_ = v[0], hi_ = v[1];
      const float t1_ = lo_ + dpp<XOR2>(lo_), t2_ = hi_ + dpp<XOR2>(hi_);
      v[0] = (lane & 2) ? t2_ : t1_;
    }
    red[wave * 64 + lane] = v[0];
  }
  {
    u32x4* z = reinterpret_cast<u32x4*>(ws + WS_ZERO);
    const int n16 = (int)(ZERO_BYTES / 16);
    for (int i = blockIdx.x * 512 + tid; i < n16; i += PRE_BLOCKS * 512) z[i] = u32x4{0u, 0u, 0u, 0u};
  }
  __syncthreads();
  if (tid < 64) {
    float s = 0.f;
#pragma unroll
    for (int wv = 0; wv < 8; ++wv) s += red[wv * 64 + tid];
    if (isq) {
      const int vi = tid >> 1, bb = vi >> 3, k = vi & 7;
      bf16* q = reinterpret_cast<bf16*>(ws + WS_Q);
      q[bb * (HQ * DH) + blk * 16 + (tid & 1) * 8 + k] = __float2bfloat16(s);
    } else {
      const float u = __shfl_xor(s, 32);
      if (tid < 32) {
        bf16* h = reinterpret_cast<bf16*>(ws + WS_H);
        const float gr = fmaxf(bfround(s), 0.f), ur = bfround(u);
        h[(tid >> 3) * FF + blk * 8 + (tid & 7)] = __float2bfloat16(gr * ur);
      }
    }
  }
}

constexpr int L_P = 0;
constexpr int L_MRG = L_P + 2048;
constexpr int MREC = 132;
constexpr int L_OB = L_MRG + NWAVE * 4 * MREC;
constexpr int L_RED = L_OB + 64;
constexpr int L_TOTAL = L_RED + 4096;

__global__ void __launch_bounds__(512, 2) k_main(const bf16* __restrict__ x, const bf16* __restrict__ kc,
                                                  const bf16* __restrict__ vc, const bf16* __restrict__ wo,
                                                  const bf16* __restrict__ wd, const bf16* __restrict__ gw,
                                                  bf16* __restrict__ out, unsigned char* __restrict__ ws) {
  __shared__ __attribute__((aligned(16))) float lds[L_TOTAL];
  const int tid = threadIdx.x, lane = tid & 63, wave = __builtin_amdgcn_readfirstlane(tid >> 6);
  const int r = lane >> 4, c = lane & 15;
  const int id = blockIdx.x, kvh = id & 7, b = (id >> 3) >> 3, s = (id >> 3) & 7;
  const int grp = b * 8 + kvh, src = kvh * 8 + s;
  constexpr float C2 = 0.088388347648318440f * 1.4426950408889634f;
#define ST(i)
  ST(0);

  const bf16* qp = reinterpret_cast<const bf16*>(ws + WS_Q) + (size_t)b * (HQ * DH) + kvh * 4 * DH + 8 * c;
  u32x4 q[4];
#pragma unroll
  for (int g = 0; g < 4; ++g) q[g] = ld16(qp + g * DH);
  const size_t kvoff = ((size_t)(b * HKV + kvh) * SK + (SK - WIN) + s * KEYS_WG + wave * KEYS_WAVE + r) * DH + 8 * c;
  u32x4 K[16], V[16];
#pragma unroll
  for (int i = 0; i < 16; ++i) K[i] = ld16nt(kc + kvoff + (size_t)((i + grp) & 15) * 4 * DH);
#pragma unroll
  for (int i = 0; i < 16; ++i) V[i] = ld16nt(vc + kvoff + (size_t)((i + grp) & 15) * 4 * DH);
  __builtin_amdgcn_sched_barrier(0);

  float sc[4];
#pragma unroll
  for (int pg = 0; pg < 4; ++pg) {
    float xx[16];
#pragma unroll
    for (int t = 0; t < 4; ++t)
#pragma unroll
      for (int g = 0; g < 4; ++g) {
        float d = 0.f;
#pragma unroll
        for (int k = 0; k < 4; ++k) d = dot2(K[pg * 4 + t][k], q[g][k], d);
        xx[t * 4 + g] = d;
      }
#define TRS(HALF, CTRL, BIT) _Pragma("unroll") for (int h = 0; h < (HALF); ++h) { \
    const float lo_ = xx[h], hi_ = xx[h + (HALF)]; const float t1_ = lo_ + dpp<CTRL>(lo_), t2_ = hi_ + dpp<CTRL>(hi_); \
    xx[h] = (c & (BIT)) ? t2_ : t1_; }
    TRS(8, XOR8, 8) TRS(4, XOR7, 4) TRS(2, XOR2, 2) TRS(1, XOR1, 1)
#undef TRS
    sc[pg] = xx[0] * C2;
  }
  ST(1);
  const int hs = s >> 1, half = s & 1;
  const bf16* wop = wo + ((size_t)((kvh * 4 + hs) * DH + half * 64 + wave * 8)) * DM + 8 * lane;
  u32x4 WO[8];
#pragma unroll
  for (int i = 0; i < 8; ++i) WO[i] = ld16nt(wop + (size_t)i * DM);
  const int drow0 = src * 28 + (wave < 7 ? wave * 4 : 0);
  u32x4 WD[4];
#pragma unroll
  for (int i = 0; i < 4; ++i) WD[i] = ld16nt(wd + (size_t)(drow0 + i) * DM + 8 * lane);
  const bf16* hp = reinterpret_cast<const bf16*>(ws + WS_H) + (size_t)b * FF + drow0;
  float hv[4];
#pragma unroll
  for (int i = 0; i < 4; ++i) hv[i] = __bfloat162float(hp[i]);
  float gsum = __bfloat162float(x[b * DM + tid]) * __bfloat162float(gw[tid]);

  float m = fmaxf(fmaxf(sc[0], sc[1]), fmaxf(sc[2], sc[3]));
  m = fmaxf(m, dpp<ROR4>(m)); m = fmaxf(m, dpp<XOR8>(m)); m = xrow16_max(m);
  float l = 0.f;
#pragma unroll
  for (int pg = 0; pg < 4; ++pg) {
    const float p = __builtin_amdgcn_exp2f(sc[pg] - m);
    l += p;
    lds[L_P + wave * 256 + (16 * pg + 4 * (c >> 2) + r) * 4 + (c & 3)] = p;
  }
  l += dpp<ROR4>(l); l += dpp<XOR8>(l); l = xrow16_sum(l);
  asm volatile("s_waitcnt lgkmcnt(0)" ::: "memory");
  float acc[32];
#pragma unroll
  for (int k = 0; k < 32; ++k) acc[k] = 0.f;
#pragma unroll
  for (int i = 0; i < 16; ++i) {
    const f32x4 p = *reinterpret_cast<const f32x4*>(&lds[L_P + wave * 256 + (4 * i + r) * 4]);
    float vf[8];
#pragma unroll
    for (int k = 0; k < 4; ++k) { vf[2 * k] = bflo(V[i][k]); vf[2 * k + 1] = bfhi(V[i][k]); }
#pragma unroll
    for (int g = 0; g < 4; ++g)
#pragma unroll
      for (int k = 0; k < 8; ++k) acc[g * 8 + k] = fmaf(p[g], vf[k], acc[g * 8 + k]);
  }
#pragma unroll
  for (int k = 0; k < 32; ++k) acc[k] = xrow16_sum(acc[k]);
  {
    float* mr = &lds[L_MRG + wave * 4 * MREC];
    if (r == 0) {
#pragma unroll
      for (int g = 0; g < 4; ++g) {
        *reinterpret_cast<f32x4*>(mr + g * MREC + 4 + 8 * c) = f32x4{acc[g * 8], acc[g * 8 + 1], acc[g * 8 + 2], acc[g * 8 + 3]};
        *reinterpret_cast<f32x4*>(mr + g * MREC + 8 + 8 * c) = f32x4{acc[g * 8 + 4], acc[g * 8 + 5], acc[g * 8 + 6], acc[g * 8 + 7]};
      }
      if (c < 4) { mr[c * MREC] = m; mr[c * MREC + 1] = l; }
    }
  }
  ST(2);
  gsum = wave_sum(gsum);
  if (lane == 0) lds[L_RED + wave] = gsum;
  __syncthreads();
  float gate;
  {
    float t = 0.f;
#pragma unroll
    for (int wv = 0; wv < 8; ++wv) t += lds[L_RED + wv];
    gate = 1.f / (1.f + __expf(-t));
  }
  u64* P1 = reinterpret_cast<u64*>(ws + WS_P1);
  u64* ML = reinterpret_cast<u64*>(ws + WS_ML);
  {
    const int g = tid >> 7, d = tid & 127;
    float M = lds[L_MRG + g * MREC];
#pragma unroll
    for (int wv = 1; wv < 8; ++wv) M = fmaxf(M, lds[L_MRG + (wv * 4 + g) * MREC]);
    float L = 0.f, o = 0.f;
#pragma unroll
    for (int wv = 0; wv < 8; ++wv) {
      const float* rec = &lds[L_MRG + (wv * 4 + g) * MREC];
      const float wt = __builtin_amdgcn_exp2f(rec[0] - M);
      L = fmaf(wt, rec[1], L); o = fmaf(wt, rec[4 + d], o);
    }
    st_gran(P1 + ((size_t)(grp * NSPLIT + s) * 4 + g) * DH + d, o);
    if (d == 0) {
      st_gran(ML + ((size_t)(grp * NSPLIT + s) * 4 + g) * 2, M);
      st_gran(ML + ((size_t)(grp * NSPLIT + s) * 4 + g) * 2 + 1, L);
    }
  }
  ST(3);
  if (wave == 0) {
    const u64* g1 = P1 + ((size_t)(grp * NSPLIT) * 4 + hs) * DH + half * 64 + lane;
    const u64* g2 = ML + ((size_t)(grp * NSPLIT + (lane >> 1 & 7)) * 4 + hs) * 2 + (lane & 1);
    float a[8], ml = 0.f;
    for (u32 spins = 0; spins < SPIN_CAP; ++spins) {
      bool ok = true;
#pragma unroll
      for (int j = 0; j < 8; ++j) {
        const u64 v = ld_gran(g1 + (size_t)j * 4 * DH);
        a[j] = __uint_as_float((u32)v); ok &= (u32)(v >> 32) == TAG;
      }
      const u64 v = ld_gran(g2);
      ml = __uint_as_float((u32)v); ok &= (u32)(v >> 32) == TAG;
      if (__all(ok)) break;
      __builtin_amdgcn_s_sleep(1);
    }
    float M = -INFINITY;
#pragma unroll
    for (int j = 0; j < 8; ++j) M = fmaxf(M, __shfl(ml, 2 * j));
    float L = 0.f, o = 0.f;
#pragma unroll
    for (int j = 0; j < 8; ++j) {
      const float wt = __builtin_amdgcn_exp2f(__shfl(ml, 2 * j) - M);
      L = fmaf(wt, __shfl(ml, 2 * j + 1), L); o = fmaf(wt, a[j], o);
    }
    lds[L_OB + lane] = bfround(o / L) * gate;
  }
  __syncthreads();
  ST(4);
  float pr[8];
#pragma unroll
  for (int k = 0; k < 8; ++k) pr[k] = 0.f;
#pragma unroll
  for (int i = 0; i < 8; ++i) {
    const float ov = lds[L_OB + wave * 8 + i];
#pragma unroll
    for (int k = 0; k < 4; ++k) {
      pr[2 * k] = fmaf(ov, bflo(WO[i][k]), pr[2 * k]);
      pr[2 * k + 1] = fmaf(ov, bfhi(WO[i][k]), pr[2 * k + 1]);
    }
  }
  if (wave < 7) {
#pragma unroll
    for (int i = 0; i < 4; ++i) {
      const float hvv = hv[i] * (1.f - gate);
#pragma unroll
      for (int k = 0; k < 4; ++k) {
        pr[2 * k] = fmaf(hvv, bflo(WD[i][k]), pr[2 * k]);
        pr[2 * k + 1] = fmaf(hvv, bfhi(WD[i][k]), pr[2 * k + 1]);
      }
    }
  }
  *reinterpret_cast<f32x4*>(&lds[L_RED + wave * 512 + 8 * lane]) = f32x4{pr[0], pr[1], pr[2], pr[3]};
  *reinterpret_cast<f32x4*>(&lds[L_RED + wave * 512 + 8 * lane + 4]) = f32x4{pr[4], pr[5], pr[6], pr[7]};
  __syncthreads();
  u64* P2 = reinterpret_cast<u64*>(ws + WS_P2);
  {
    float t = 0.f;
#pragma unroll
    for (int wv = 0; wv < 8; ++wv) t += lds[L_RED + wv * 512 + tid];
    st_gran(P2 + ((size_t)(b * 64 + src)) * DM + tid, t);
  }
  ST(5);
  if (wave == 0) {
    const u64* g = P2 + ((size_t)(b * 64 + lane)) * DM + 8 * src;
    float a[8];
    for (u32 spins = 0; spins < SPIN_CAP; ++spins) {
      bool ok = true;
#pragma unroll
      for (int j = 0; j < 8; ++j) {
        const u64 v = ld_gran(g + j);
        a[j] = __uint_as_float((u32)v); ok &= (u32)(v >> 32) == TAG;
      }
      if (__all(ok)) break;
      __builtin_amdgcn_s_sleep(1);
    }
#pragma unroll
    for (int j = 0; j < 8; ++j) a[j] = wave_sum(a[j]);
    if (lane == 0) {
      u32x4 o;
#pragma unroll
      for (int k = 0; k < 4; ++k) o[k] = f2bf_bits(a[2 * k]) | (f2bf_bits(a[2 * k + 1]) << 16);
      *reinterpret_cast<u32x4*>(out + (size_t)b * DM + 8 * src) = o;
    }
    ST(6);
  }
}

extern "C" void kernel_launch(void* const* d_in, const int* in_sizes, int n_in,
                              void* d_out, int out_size, void* d_ws, size_t ws_size,
                              hipStream_t stream) {
  const bf16* x = (const bf16*)d_in[0];
  const bf16* kc = (const bf16*)d_in[1];
  const bf16* vc = (const bf16*)d_in[2];
  const bf16* wq = (const bf16*)d_in[3];
  const bf16* wo = (const bf16*)d_in[4];
  const bf16* wg = (const bf16*)d_in[6];
  const bf16* wu = (const bf16*)d_in[7];
  const bf16* wd = (const bf16*)d_in[8];
  const bf16* gw = (const bf16*)d_in[9];
  unsigned char* ws = (unsigned char*)d_ws;
  k_pre<<<PRE_BLOCKS, 512, 0, stream>>>(x, wq, wg, wu, ws);
  k_main<<<NWG, 512, 0, stream>>>(x, kc, vc, wo, wd, gw, (bf16*)d_out, ws);
}
```

```cpp
#include <hip/hip_runtime.h>
#include <hip/hip_bf16.h>
#include <stdint.h>

typedef __hip_bfloat16 bf16;
typedef unsigned int u32;
typedef unsigned long long u64;
typedef u32 u32x4 __attribute__((ext_vector_type(4)));
typedef float f32x4 __attribute__((ext_vector_type(4)));
typedef __bf16 bf16x2 __attribute__((ext_vector_type(2)));
#define GAS __attribute__((address_space(1)))
typedef GAS u64 gu64;

constexpr int NB = 4, DM = 512, HQ = 32, HKV = 8, SK = 16384, DH = 128, WIN = 4096, FF = 1792;
constexpr int NSPLIT = 8, KEYS_WG = WIN / NSPLIT;
constexpr int NWAVE = 8, KEYS_WAVE = KEYS_WG / NWAVE;
constexpr int NWG = NB * HKV * NSPLIT;

constexpr size_t WS_Q = 0;
constexpr size_t WS_H = WS_Q + (size_t)NB * HQ * DH * 2;
constexpr size_t WS_ZERO = 64 * 1024;
constexpr size_t WS_ML = WS_ZERO;
constexpr size_t ML_BYTES = (size_t)32 * NSPLIT * 4 * 2 * 8;
constexpr size_t WS_P1 = WS_ML + ML_BYTES;
constexpr size_t P1_BYTES = (size_t)32 * NSPLIT * 4 * DH * 8;
constexpr size_t WS_P2 = WS_P1 + P1_BYTES;
constexpr size_t P2_BYTES = (size_t)NB * 64 * DM * 8;
constexpr size_t ZERO_BYTES = ML_BYTES + P1_BYTES + P2_BYTES;
constexpr int PRE_QBLK = HQ * DH / 16;
constexpr int PRE_HBLK = FF / 16;
constexpr int PRE_BLOCKS = PRE_QBLK + PRE_HBLK;
constexpr u32 TAG = 0x5A5A0001u;
constexpr u32 SPIN_CAP = 1u << 20;

#define RLX_AGENT __ATOMIC_RELAXED, __HIP_MEMORY_SCOPE_AGENT

__device__ __forceinline__ float bf2f(u32 lo16) { return __uint_as_float(lo16 << 16); }
__device__ __forceinline__ float bflo(u32 p) { return __uint_as_float(p << 16); }
__device__ __forceinline__ float bfhi(u32 p) { return __uint_as_float(p & 0xffff0000u); }
__device__ __forceinline__ u32 f2bf_bits(float f) {
  bf16 h = __float2bfloat16(f);
  return (u32)__builtin_bit_cast(unsigned short, h);
}
__device__ __forceinline__ float bfround(float f) { return __uint_as_float(f2bf_bits(f) << 16); }
__device__ __forceinline__ float dot2(u32 a, u32 b, float c) {
  return __builtin_amdgcn_fdot2_f32_bf16(__builtin_bit_cast(bf16x2, a), __builtin_bit_cast(bf16x2, b), c, false);
}
template <int CTRL> __device__ __forceinline__ float dpp(float x) {
  return __builtin_bit_cast(float, __builtin_amdgcn_mov_dpp(__builtin_bit_cast(int, x), CTRL, 0xf, 0xf, true));
}
constexpr int XOR1 = 0xB1, XOR2 = 0x4E, XOR7 = 0x141, XOR8 = 0x128, ROR4 = 0x124;
__device__ __forceinline__ float xrow16_max(float x) {
  auto s = __builtin_amdgcn_permlane16_swap(__float_as_uint(x), __float_as_uint(x), false, false);
  x = fmaxf(__uint_as_float(s[0]), __uint_as_float(s[1]));
  auto t = __builtin_amdgcn_permlane32_swap(__float_as_uint(x), __float_as_uint(x), false, false);
  return fmaxf(__uint_as_float(t[0]), __uint_as_float(t[1]));
}
__device__ __forceinline__ float xrow16_sum(float x) {
  auto s = __builtin_amdgcn_permlane16_swap(__float_as_uint(x), __float_as_uint(x), false, false);
  x = __uint_as_float(s[0]) + __uint_as_float(s[1]);
  auto t = __builtin_amdgcn_permlane32_swap(__float_as_uint(x), __float_as_uint(x), false, false);
  return __uint_as_float(t[0]) + __uint_as_float(t[1]);
}
__device__ __forceinline__ float wave_sum(float x) {
  x += dpp<XOR1>(x); x += dpp<XOR2>(x); x += dpp<XOR7>(x); x += dpp<XOR8>(x);
  return xrow16_sum(x);
}
__device__ __forceinline__ u32x4 ld16(const void* p) { return *reinterpret_cast<const u32x4*>(p); }
__device__ __forceinline__ u32x4 ld16nt(const void* p) { return __builtin_nontemporal_load(reinterpret_cast<const u32x4*>(p)); }
__device__ __forceinline__ void st_gran(u64* g, float v) {
  __hip_atomic_store((gu64*)g, ((u64)TAG << 32) | (u64)__float_as_uint(v), RLX_AGENT);
}
__device__ __forceinline__ u64 ld_gran(const u64* g) { return __hip_atomic_load((gu64*)g, RLX_AGENT); }

#define TRS_DPP(ARR, HALF, CTRL, BIT, LANE) _Pragma("unroll") for (int h = 0; h < (HALF); ++h) { \
    const float lo_ = ARR[h], hi_ = ARR[h + (HALF)]; const float t1_ = lo_ + dpp<CTRL>(lo_), t2_ = hi_ + dpp<CTRL>(hi_); \
    ARR[h] = ((LANE) & (BIT)) ? t2_ : t1_; }
#define TRS_P32(ARR, HALF) _Pragma("unroll") for (int h = 0; h < (HALF); ++h) { \
    auto s_ = __builtin_amdgcn_permlane32_swap(__float_as_uint(ARR[h]), __float_as_uint(ARR[h + (HALF)]), false, false); \
    ARR[h] = __uint_as_float(s_[0]) + __uint_as_float(s_[1]); }
#define TRS_P16(ARR, HALF) _Pragma("unroll") for (int h = 0; h < (HALF); ++h) { \
    auto s_ = __builtin_amdgcn_permlane16_swap(__float_as_uint(ARR[h]), __float_as_uint(ARR[h + (HALF)]), false, false); \
    ARR[h] = __uint_as_float(s_[0]) + __uint_as_float(s_[1]); }

__device__ __forceinline__ float slice16(const u32x4 w0, const u32x4 w1, const float (&x0)[4], const float (&x1)[4], int lane) {
  float v[32];
#pragma unroll
  for (int b = 0; b < 4; ++b)
#pragma unroll
    for (int j = 0; j < 4; ++j) {
      v[b * 8 + 2 * j] = x0[b] * bflo(w0[j]) + x1[b] * bflo(w1[j]);
      v[b * 8 + 2 * j + 1] = x0[b] * bfhi(w0[j]) + x1[b] * bfhi(w1[j]);
    }
  TRS_P32(v, 16) TRS_P16(v, 8) TRS_DPP(v, 4, XOR8, 8, lane)
#pragma unroll
  for (int h = 0; h < 2; ++h) {
    const float lo_ = v[h], hi_ = v[h + 2];
    const float t1_ = lo_ + __shfl_xor(lo_, 4), t2_ = hi_ + __shfl_xor(hi_, 4);
    v[h] = (lane & 4) ? t2_ : t1_;
  }
  const float lo_ = v[0], hi_ = v[1];
  const float t1_ = lo_ + dpp<XOR2>(lo_), t2_ = hi_ + dpp<XOR2>(hi_);
  return (lane & 2) ? t2_ : t1_;
}

__global__ void __launch_bounds__(512) k_pre(const bf16* __restrict__ x, const bf16* __restrict__ wq,
                                              const bf16* __restrict__ wg, const bf16* __restrict__ wu,
                                              unsigned char* __restrict__ ws) {
  __shared__ __attribute__((aligned(16))) float red[1024];
  const int tid = threadIdx.x, lane = tid & 63, wave = tid >> 6;
  const bool isq = blockIdx.x >= PRE_HBLK;
  const int bq = blockIdx.x - PRE_HBLK;
  const int blk = isq ? (bq & 7) * 32 + (bq >> 3) : (blockIdx.x & 7) * 14 + (blockIdx.x >> 3);
  const int r0 = ((tid >> 1) + blk * 32) & 255;
  const int ld = isq ? HQ * DH : FF;
  const size_t off = (size_t)r0 * ld + blk * 16 + (tid & 1) * 8;
  const bf16* wa = isq ? wq : wg;
  const u32x4 w0 = ld16nt(wa + off), w1 = ld16nt(wa + off + (size_t)256 * ld);
  u32x4 w2 = w0, w3 = w1;
  if (!isq) { w2 = ld16nt(wu + off); w3 = ld16nt(wu + off + (size_t)256 * ld); }
  float x0[4], x1[4];
#pragma unroll
  for (int b = 0; b < 4; ++b) { x0[b] = __bfloat162float(x[b * DM + r0]); x1[b] = __bfloat162float(x[b * DM + r0 + 256]); }
  red[wave * 64 + lane] = slice16(w0, w1, x0, x1, lane);
  if (!isq) red[512 + wave * 64 + lane] = slice16(w2, w3, x0, x1, lane);
  {
    u32x4* z = reinterpret_cast<u32x4*>(ws + WS_ZERO);
    const int n16 = (int)(ZERO_BYTES / 16);
    for (int i = blockIdx.x * 512 + tid; i < n16; i += PRE_BLOCKS * 512) z[i] = u32x4{0u, 0u, 0u, 0u};
  }
  __syncthreads();
  if (tid < 64) {
    float s = 0.f, u = 0.f;
#pragma unroll
    for (int wv = 0; wv < 8; ++wv) s += red[wv * 64 + tid];
    const int vi = tid >> 1, col = blk * 16 + (tid & 1) * 8 + (vi & 7), bb = vi >> 3;
    if (isq) {
      reinterpret_cast<bf16*>(ws + WS_Q)[bb * (HQ * DH) + col] = __float2bfloat16(s);
    } else {
#pragma unroll
      for (int wv = 0; wv < 8; ++wv) u += red[512 + wv * 64 + tid];
      const float gr = fmaxf(bfround(s), 0.f), ur = bfround(u);
      reinterpret_cast<bf16*>(ws + WS_H)[bb * FF + col] = __float2bfloat16(gr * ur);
    }
  }
}

constexpr int L_P = 0;
constexpr int L_MRG = L_P + 2048;
constexpr int MREC = 132;
constexpr int L_OB = L_MRG + NWAVE * 4 * MREC;
constexpr int L_RED = L_OB + 64;
constexpr int L_TOTAL = L_RED + 4096;

__global__ void __launch_bounds__(512, 2) k_main(const bf16* __restrict__ x, const bf16* __restrict__ kc,
                                                  const bf16* __restrict__ vc, const bf16* __restrict__ wo,
                                                  const bf16* __restrict__ wd, const bf16* __restrict__ gw,
                                                  bf16* __restrict__ out, unsigned char* __restrict__ ws) {
  __shared__ __attribute__((aligned(16))) float lds[L_TOTAL];
  const int tid = threadIdx.x, lane = tid & 63, wave = __builtin_amdgcn_readfirstlane(tid >> 6);
  const int r = lane >> 4, c = lane & 15;
  const int id = blockIdx.x, kvh = id & 7, b = (id >> 3) >> 3, s = (id >> 3) & 7;
  const int grp = b * 8 + kvh, src = kvh * 8 + s;
  constexpr float C2 = 0.088388347648318440f * 1.4426950408889634f;
#define ST(i)
  ST(0);

  const bf16* qp = reinterpret_cast<const bf16*>(ws + WS_Q) + (size_t)b * (HQ * DH) + kvh * 4 * DH + 8 * c;
  u32x4 q[4];
#pragma unroll
  for (int g = 0; g < 4; ++g) q[g] = ld16(qp + g * DH);
  const size_t kvoff = ((size_t)(b * HKV + kvh) * SK + (SK - WIN) + s * KEYS_WG + wave * KEYS_WAVE + r) * DH + 8 * c;
  u32x4 K[16], V[16];
#pragma unroll
  for (int i = 0; i < 16; ++i) K[i] = ld16nt(kc + kvoff + (size_t)((i + grp) & 15) * 4 * DH);
#pragma unroll
  for (int i = 0; i < 16; ++i) V[i] = ld16nt(vc + kvoff + (size_t)((i + grp) & 15) * 4 * DH);
  __builtin_amdgcn_sched_barrier(0);

  float sc[4];
#pragma unroll
  for (int pg = 0; pg < 4; ++pg) {
    float xx[16];
#pragma unroll
    for (int t = 0; t < 4; ++t)
#pragma unroll
      for (int g = 0; g < 4; ++g) {
        float d = 0.f;
#pragma unroll
        for (int k = 0; k < 4; ++k) d = dot2(K[pg * 4 + t][k], q[g][k], d);
        xx[t * 4 + g] = d;
      }
#define TRS(HALF, CTRL, BIT) _Pragma("unroll") for (int h = 0; h < (HALF); ++h) { \
    const float lo_ = xx[h], hi_ = xx[h + (HALF)]; const float t1_ = lo_ + dpp<CTRL>(lo_), t2_ = hi_ + dpp<CTRL>(hi_); \
    xx[h] = (c & (BIT)) ? t2_ : t1_; }
    TRS(8, XOR8, 8) TRS(4, XOR7, 4) TRS(2, XOR2, 2) TRS(1, XOR1, 1)
#undef TRS
    sc[pg] = xx[0] * C2;
  }
  ST(1);
  const int hs = s >> 1, half = s & 1;
  const bf16* wop = wo + ((size_t)((kvh * 4 + hs) * DH + half * 64 + wave * 8)) * DM + 8 * lane;
  u32x4 WO[8];
#pragma unroll
  for (int i = 0; i < 8; ++i) WO[i] = ld16nt(wop + (size_t)i * DM);
  const int drow0 = src * 28 + (wave < 7 ? wave * 4 : 0);
  u32x4 WD[4];
#pragma unroll
  for (int i = 0; i < 4; ++i) WD[i] = ld16nt(wd + (size_t)(drow0 + i) * DM + 8 * lane);
  const bf16* hp = reinterpret_cast<const bf16*>(ws + WS_H) + (size_t)b * FF + drow0;
  float hv[4];
#pragma unroll
  for (int i = 0; i < 4; ++i) hv[i] = __bfloat162float(hp[i]);
  float gsum = __bfloat162float(x[b * DM + tid]) * __bfloat162float(gw[tid]);

  float m = fmaxf(fmaxf(sc[0], sc[1]), fmaxf(sc[2], sc[3]));
  m = fmaxf(m, dpp<ROR4>(m)); m = fmaxf(m, dpp<XOR8>(m)); m = xrow16_max(m);
  float l = 0.f;
#pragma unroll
  for (int pg = 0; pg < 4; ++pg) {
    const float p = __builtin_amdgcn_exp2f(sc[pg] - m);
    l += p;
    lds[L_P + wave * 256 + (16 * pg + 4 * (c >> 2) + r) * 4 + (c & 3)] = p;
  }
  l += dpp<ROR4>(l); l += dpp<XOR8>(l); l = xrow16_sum(l);
  asm volatile("s_waitcnt lgkmcnt(0)" ::: "memory");
  float acc[32];
#pragma unroll
  for (int k = 0; k < 32; ++k) acc[k] = 0.f;
#pragma unroll
  for (int i = 0; i < 16; ++i) {
    const f32x4 p = *reinterpret_cast<const f32x4*>(&lds[L_P + wave * 256 + (4 * i + r) * 4]);
    float vf[8];
#pragma unroll
    for (int k = 0; k < 4; ++k) { vf[2 * k] = bflo(V[i][k]); vf[2 * k + 1] = bfhi(V[i][k]); }
#pragma unroll
    for (int g = 0; g < 4; ++g)
#pragma unroll
      for (int k = 0; k < 8; ++k) acc[g * 8 + k] = fmaf(p[g], vf[k], acc[g * 8 + k]);
  }
#pragma unroll
  for (int k = 0; k < 32; ++k) acc[k] = xrow16_sum(acc[k]);
  {
    float* mr = &lds[L_MRG + wave * 4 * MREC];
    if (r == 0) {
#pragma unroll
      for (int g = 0; g < 4; ++g) {
        *reinterpret_cast<f32x4*>(mr + g * MREC + 4 + 8 * c) = f32x4{acc[g * 8], acc[g * 8 + 1], acc[g * 8 + 2], acc[g * 8 + 3]};
        *reinterpret_cast<f32x4*>(mr + g * MREC + 8 + 8 * c) = f32x4{acc[g * 8 + 4], acc[g * 8 + 5], acc[g * 8 + 6], acc[g * 8 + 7]};
      }
      if (c < 4) { mr[c * MREC] = m; mr[c * MREC + 1] = l; }
    }
  }
  ST(2);
  gsum = wave_sum(gsum);
  if (lane == 0) lds[L_RED + wave] = gsum;
  __syncthreads();
  float gate;
  {
    float t = 0.f;
#pragma unroll
    for (int wv = 0; wv < 8; ++wv) t += lds[L_RED + wv];
    gate = 1.f / (1.f + __expf(-t));
  }
  u64* P1 = reinterpret_cast<u64*>(ws + WS_P1);
  u64* ML = reinterpret_cast<u64*>(ws + WS_ML);
  {
    const int g = tid >> 7, d = tid & 127;
    float M = lds[L_MRG + g * MREC];
#pragma unroll
    for (int wv = 1; wv < 8; ++wv) M = fmaxf(M, lds[L_MRG + (wv * 4 + g) * MREC]);
    float L = 0.f, o = 0.f;
#pragma unroll
    for (int wv = 0; wv < 8; ++wv) {
      const float* rec = &lds[L_MRG + (wv * 4 + g) * MREC];
      const float wt = __builtin_amdgcn_exp2f(rec[0] - M);
      L = fmaf(wt, rec[1], L); o = fmaf(wt, rec[4 + d], o);
    }
    st_gran(P1 + ((size_t)(grp * NSPLIT + s) * 4 + g) * DH + d, o);
    if (d == 0) {
      st_gran(ML + ((size_t)(grp * NSPLIT + s) * 4 + g) * 2, M);
      st_gran(ML + ((size_t)(grp * NSPLIT + s) * 4 + g) * 2 + 1, L);
    }
  }
  ST(3);
  if (wave == 0) {
    const u64* g1 = P1 + ((size_t)(grp * NSPLIT) * 4 + hs) * DH + half * 64 + lane;
    const u64* g2 = ML + ((size_t)(grp * NSPLIT + (lane >> 1 & 7)) * 4 + hs) * 2 + (lane & 1);
    float a[8], ml = 0.f;
    for (u32 spins = 0; spins < SPIN_CAP; ++spins) {
      bool ok = true;
#pragma unroll
      for (int j = 0; j < 8; ++j) {
        const u64 v = ld_gran(g1 + (size_t)j * 4 * DH);
        a[j] = __uint_as_float((u32)v); ok &= (u32)(v >> 32) == TAG;
      }
      const u64 v = ld_gran(g2);
      ml = __uint_as_float((u32)v); ok &= (u32)(v >> 32) == TAG;
      if (__all(ok)) break;
      __builtin_amdgcn_s_sleep(1);
    }
    float M = -INFINITY;
#pragma unroll
    for (int j = 0; j < 8; ++j) M = fmaxf(M, __shfl(ml, 2 * j));
    float L = 0.f, o = 0.f;
#pragma unroll
    for (int j = 0; j < 8; ++j) {
      const float wt = __builtin_amdgcn_exp2f(__shfl(ml, 2 * j) - M);
      L = fmaf(wt, __shfl(ml, 2 * j + 1), L); o = fmaf(wt, a[j], o);
    }
    lds[L_OB + lane] = bfround(o / L) * gate;
  }
  __syncthreads();
  ST(4);
  float pr[8];
#pragma unroll
  for (int k = 0; k < 8; ++k) pr[k] = 0.f;
#pragma unroll
  for (int i = 0; i < 8; ++i) {
    const float ov = lds[L_OB + wave * 8 + i];
#pragma unroll
    for (int k = 0; k < 4; ++k) {
      pr[2 * k] = fmaf(ov, bflo(WO[i][k]), pr[2 * k]);
      pr[2 * k + 1] = fmaf(ov, bfhi(WO[i][k]), pr[2 * k + 1]);
    }
  }
  if (wave < 7) {
#pragma unroll
    for (int i = 0; i < 4; ++i) {
      const float hvv = hv[i] * (1.f - gate);
#pragma unroll
      for (int k = 0; k < 4; ++k) {
        pr[2 * k] = fmaf(hvv, bflo(WD[i][k]), pr[2 * k]);
        pr[2 * k + 1] = fmaf(hvv, bfhi(WD[i][k]), pr[2 * k + 1]);
      }
    }
  }
  *reinterpret_cast<f32x4*>(&lds[L_RED + wave * 512 + 8 * lane]) = f32x4{pr[0], pr[1], pr[2], pr[3]};
  *reinterpret_cast<f32x4*>(&lds[L_RED + wave * 512 + 8 * lane + 4]) = f32x4{pr[4], pr[5], pr[6], pr[7]};
  __syncthreads();
  u64* P2 = reinterpret_cast<u64*>(ws + WS_P2);
  {
    float t = 0.f;
#pragma unroll
    for (int wv = 0; wv < 8; ++wv) t += lds[L_RED + wv * 512 + tid];
    st_gran(P2 + ((size_t)(b * 64 + src)) * DM + tid, t);
  }
  ST(5);
  if (wave == 0) {
    const u64* g = P2 + ((size_t)(b * 64 + lane)) * DM + 8 * src;
    float a[8];
    for (u32 spins = 0; spins < SPIN_CAP; ++spins) {
      bool ok = true;
#pragma unroll
      for (int j = 0; j < 8; ++j) {
        const u64 v = ld_gran(g + j);
        a[j] = __uint_as_float((u32)v); ok &= (u32)(v >> 32) == TAG;
      }
      if (__all(ok)) break;
      __builtin_amdgcn_s_sleep(1);
    }
#pragma unroll
    for (int j = 0; j < 8; ++j) a[j] = wave_sum(a[j]);
    if (lane == 0) {
      u32x4 o;
#pragma unroll
      for (int k = 0; k < 4; ++k) o[k] = f2bf_bits(a[2 * k]) | (f2bf_bits(a[2 * k + 1]) << 16);
      *reinterpret_cast<u32x4*>(out + (size_t)b * DM + 8 * src) = o;
    }
    ST(6);
  }
}

extern "C" void kernel_launch(void* const* d_in, const int* in_sizes, int n_in,
                              void* d_out, int out_size, void* d_ws, size_t ws_size,
                              hipStream_t stream) {
  const bf16* x = (const bf16*)d_in[0];
  const bf16* kc = (const bf16*)d_in[1];
  const bf16* vc = (const bf16*)d_in[2];
  const bf16* wq = (const bf16*)d_in[3];
  const bf16* wo = (const bf16*)d_in[4];
  const bf16* wg = (const bf16*)d_in[6];
  const bf16* wu = (const bf16*)d_in[7];
  const bf16* wd = (const bf16*)d_in[8];
  const bf16* gw = (const bf16*)d_in[9];
  unsigned char* ws = (unsigned char*)d_ws;

  k_pre<<<PRE_BLOCKS, 512, 0, stream>>>(x, wq, wg, wu, ws);
  k_main<<<NWG, 512, 0, stream>>>(x, kc, vc, wo, wd, gw, (bf16*)d_out, ws);
}
```
